```python
import torch

def _softmax(x, axis=-1):
    return torch.softmax(x, dim=axis)
def _gelu(x):
    return torch.nn.functional.gelu(x, approximate="tanh")
def _silu(x):
    return torch.nn.functional.silu(x)
def _sigmoid(x):
    return torch.sigmoid(x)
def _layernorm(x, axis=-1, eps=1e-5):
    m = x.mean(dim=axis, keepdim=True)
    v = x.var(dim=axis, keepdim=True, unbiased=False)
    return (x - m) / torch.sqrt(v + eps)
def _scatter_add(tgt, idx, src, axis=0):
    return tgt.clone().index_add_(axis, idx, src)
def _segment_sum(src, seg, n):
    out = torch.zeros((n,) + src.shape[1:], dtype=src.dtype)
    return out.index_add_(0, seg, src)
def _pad(x, pw):
    flat = tuple(v for pair in reversed(pw) for v in pair)
    return torch.nn.functional.pad(x, flat)

kernel_name = 'recurrent_scan__linear_attn_block__geglu_ffn__prompt'

def setup_inputs():

    _w = (lambda a: (lambda t: t.to(torch.bfloat16) if t.is_floating_point() else t)(torch.as_tensor(a)))
    def _A_setup():
        B, S, D = 1, 2048, 512
        return {
            "x": _w((lambda *s: torch.randn(*s, dtype=torch.bfloat16) * 0.05)(B, S, D)),
            "wqkv": _w((lambda *s: torch.randn(*s, dtype=torch.bfloat16) * 0.05)(D, 3 * D)),
            "wo": _w((lambda *s: torch.randn(*s, dtype=torch.bfloat16) * 0.05)(D, D)),
        }
    _A_in = _A_setup()

    def _B_setup():
        B, S, D, F = 1, 2048, 512, 1792
        return {
            "x": _w((lambda *s: torch.randn(*s, dtype=torch.bfloat16) * 0.05)(B, S, D)),
            "w_gate": _w((lambda *s: torch.randn(*s, dtype=torch.bfloat16) * 0.05)(D, F)),
            "w_up": _w((lambda *s: torch.randn(*s, dtype=torch.bfloat16) * 0.05)(D, F)),
            "w_down": _w((lambda *s: torch.randn(*s, dtype=torch.bfloat16) * 0.05)(F, D)),
        }
    _B_in = _B_setup()

    return {
        "A_x": _A_in["x"],
        "A_wqkv": _A_in["wqkv"],
        "A_wo": _A_in["wo"],
        "B_x": _B_in["x"],
        "B_w_gate": _B_in["w_gate"],
        "B_w_up": _B_in["w_up"],
        "B_w_down": _B_in["w_down"],
    }

def reference(A_x, A_wqkv, A_wo, B_x, B_w_gate, B_w_up, B_w_down):
    def _A_ref(x, wqkv, wo):
        B, S, D = x.shape
        H = 32; Dh = D // H
        qkv = (lambda x, *d: x.permute(*d))((x @ wqkv).reshape(B, S, 3, H, Dh), 2, 0, 3, 1, 4)
        q, k, v = qkv[0], qkv[1], qkv[2]
        phi = lambda t: torch.where(t > 0, t + 1, torch.exp(t)) + 1e-6
        pq, pk = phi(q), phi(k)
        kv = torch.einsum("bhsd,bhse->bhde", pk, v)
        z = torch.einsum("bhsd->bhd", pk)
        num = torch.einsum("bhqd,bhde->bhqe", pq, kv)
        den = torch.einsum("bhqd,bhd->bhq", pq, z)[..., None] + 1e-6
        o = num / den
        return (lambda x, *d: x.permute(*d))(o, 0, 2, 1, 3).reshape(B, S, D) @ wo

    def _B_ref(x, w_gate, w_up, w_down):
        return (_gelu(x @ w_gate) * (x @ w_up)) @ w_down

    x = A_x
    xn = _layernorm(x, axis=-1)
    s = _A_ref(xn, A_wqkv, A_wo)
    g = _silu(_B_ref(xn, B_w_gate, B_w_up, B_w_down))
    return x + s * g
```

```cpp
#include <hip/hip_runtime.h>
#include <hip/hip_bf16.h>
#include <stdint.h>

typedef unsigned short u16;
typedef __attribute__((ext_vector_type(8))) short bf16x8;
typedef __attribute__((ext_vector_type(4))) short bf16x4;
typedef __attribute__((ext_vector_type(4))) float f32x4;
typedef __attribute__((ext_vector_type(2))) unsigned u32x2;
typedef __attribute__((ext_vector_type(4))) unsigned u32x4;

#define S_LEN 2048
#define D_MODEL 512
#define F_DIM 1792
#define NHEAD 32

#define WS_XN      0
#define WS_PQ      (2u*1024*1024)
#define WS_H       (4u*1024*1024)
#define WS_KVP     (12u*1024*1024)
#define WS_KVT     (13u*1024*1024)
#define WS_ZB      (13u*1024*1024 + 65536)

__device__ __forceinline__ float bf2f(u16 v) { return __uint_as_float(((unsigned)v) << 16); }
__device__ __forceinline__ u16 f2bf(float f) {
  __bf16 b = (__bf16)f;
  return __builtin_bit_cast(u16, b);
}
__device__ __forceinline__ float rbf(float f) { return bf2f(f2bf(f)); }
__device__ __forceinline__ unsigned pack2(float lo, float hi) {
  return (unsigned)f2bf(lo) | ((unsigned)f2bf(hi) << 16);
}

__device__ __forceinline__ float phi_f(float t) {
  float r = (t > 0.f) ? rbf(t + 1.0f) : rbf(__expf(t));
  return rbf(r + 1e-6f);
}
__device__ __forceinline__ float gelu_f(float x) {
  const float k0 = 0.7978845608028654f, k1 = 0.044715f;
  float u = k0 * (x + k1 * x * x * x);
  float e = __expf(-2.0f * u);
  return x * __frcp_rn(1.0f + e);
}
__device__ __forceinline__ float silu_f(float x) {
  return x * __frcp_rn(1.0f + __expf(-x));
}

__device__ __forceinline__ float wave_sum(float v) {
#pragma unroll
  for (int o = 32; o > 0; o >>= 1) v += __shfl_xor(v, o, 64);
  return v;
}

__global__ __launch_bounds__(256) void ln_kernel(const u16* __restrict__ x, u16* __restrict__ xn) {
  const int wave = threadIdx.x >> 6, lane = threadIdx.x & 63;
  const int row = blockIdx.x * 4 + wave;
  const u32x4 v = *(const u32x4*)(x + (size_t)row * D_MODEL + lane * 8);
  float f[8];
#pragma unroll
  for (int i = 0; i < 4; ++i) {
    f[2 * i] = __uint_as_float(v[i] << 16);
    f[2 * i + 1] = __uint_as_float(v[i] & 0xffff0000u);
  }
  float s = 0.f;
#pragma unroll
  for (int i = 0; i < 8; ++i) s += f[i];
  s = wave_sum(s);
  const float mean = s * (1.0f / D_MODEL);
  float ss = 0.f;
#pragma unroll
  for (int i = 0; i < 8; ++i) { float d = f[i] - mean; ss += d * d; }
  ss = wave_sum(ss);
  const float var = ss * (1.0f / D_MODEL);
  const float mb = rbf(mean);
  const float vb = rbf(var);
  const float den = rbf(sqrtf(rbf(vb + 1e-5f)));
  u32x4 o;
#pragma unroll
  for (int i = 0; i < 4; ++i) {
    float a = rbf(f[2 * i] - mb) / den;
    float b = rbf(f[2 * i + 1] - mb) / den;
    o[i] = pack2(a, b);
  }
  *(u32x4*)(xn + (size_t)row * D_MODEL + lane * 8) = o;
}

#define G1_BM 128
#define G1_BN 128
#define G1_BK 64
#define G1_NT 40
#define G1_MT 16

typedef __attribute__((address_space(3))) void lds_void;

__device__ __forceinline__ bf16x4 tr_read(const char* lds_ptr) {
  return __builtin_amdgcn_ds_read_tr16_b64_v4i16((__attribute__((address_space(3))) bf16x4*)lds_ptr);
}

template <int TYPE>
__device__ __forceinline__ void g1_body(const u16* __restrict__ xn, const u16* __restrict__ wqkv,
                                        const u16* __restrict__ wg, const u16* __restrict__ wu,
                                        u16* __restrict__ pq, u16* __restrict__ hbuf,
                                        float* __restrict__ kvp, char* smem, int mt, int j) {
  const int tid = threadIdx.x, w = tid >> 6, l = tid & 63;
  const int wr = w >> 1, wc = w & 1;
  const int g = l >> 4, l15 = l & 15;
  const int m0 = mt * G1_BM;

  const u16* aptr[4];
  const u16* bptr[4];
#pragma unroll
  for (int t = 0; t < 4; ++t) {
    int P = 4 * w + t;
    int row = 8 * P + (l >> 3);
    int chunk = (l & 7) ^ ((row >> 1) & 7);
    aptr[t] = xn + (size_t)(m0 + row) * D_MODEL + chunk * 8;
    int krow = 16 * w + 4 * t + (l >> 4);
    int f = (krow & 3) | (((krow >> 3) & 1) << 2);
    int c = (l & 15) ^ (f << 1);
    int u = c >> 1, half = c & 1;
    int uwh = u >> 2, sub = u & 3;
    const u16* base;
    int ld;
    if (TYPE == 0) {
      int head = 4 * j + 2 * uwh + (sub & 1);
      base = wqkv + ((sub < 2) ? 512 : 1024) + 16 * head;
      ld = 1536;
    } else if (TYPE == 1) {
      base = wqkv + 128 * j + 16 * u;
      ld = 1536;
    } else {
      int gi = 4 * j + 2 * uwh + (sub & 1);
      base = ((sub < 2) ? wg : wu) + 16 * gi;
      ld = F_DIM;
    }
    bptr[t] = base + (size_t)krow * ld + half * 8;
  }
  const int ldb = (TYPE == 2) ? F_DIM : 1536;

  char* sA0 = smem;
  char* sB0 = smem + 32768;

  auto stage = [&](int kt, int buf) {
    char* sA = sA0 + buf * 16384;
    char* sB = sB0 + buf * 16384;
#pragma unroll
    for (int t = 0; t < 4; ++t) {
      int P = 4 * w + t;
      __builtin_amdgcn_global_load_lds((const unsigned*)(aptr[t] + kt * G1_BK),
                                       (__attribute__((address_space(3))) unsigned*)(sA + P * 1024 + l * 16), 16, 0, 0);
    }
#pragma unroll
    for (int t = 0; t < 4; ++t) {
      int P = 4 * w + t;
      __builtin_amdgcn_global_load_lds((const unsigned*)(bptr[t] + (size_t)kt * G1_BK * ldb),
                                       (__attribute__((address_space(3))) unsigned*)(sB + P * 1024 + l * 16), 16, 0, 0);
    }
  };

  const int a_off0 = (wr * 64 + l15) * 128 + ((g ^ (l15 >> 1)) * 16);
  const int q = l15 >> 2, p = l & 3;
  const int fB = q | ((g & 1) << 2);
  int b_off[4];
#pragma unroll
  for (int nt = 0; nt < 4; ++nt) {
    int c = 8 * wc + 2 * nt + (p >> 1);
    b_off[nt] = (8 * g + q) * 256 + ((c ^ (fB << 1)) * 16) + (p & 1) * 8;
  }

  f32x4 acc[4][4];
#pragma unroll
  for (int a = 0; a < 4; ++a)
#pragma unroll
    for (int b = 0; b < 4; ++b) acc[a][b] = (f32x4){0.f, 0.f, 0.f, 0.f};

  const int NK = D_MODEL / G1_BK;
  stage(0, 0);
  for (int kt = 0; kt < NK; ++kt) {
    __syncthreads();
    if (kt + 1 < NK) stage(kt + 1, (kt + 1) & 1);
    const char* sA = sA0 + (kt & 1) * 16384;
    const char* sB = sB0 + (kt & 1) * 16384;
#pragma unroll
    for (int ks = 0; ks < 2; ++ks) {
      bf16x8 af[4], bfr[4];
#pragma unroll
      for (int rt = 0; rt < 4; ++rt)
        af[rt] = *(const bf16x8*)(sA + rt * 2048 + (a_off0 ^ (ks * 64)));
#pragma unroll
      for (int nt = 0; nt < 4; ++nt) {
        bf16x4 lo = tr_read(sB + ks * 8192 + b_off[nt]);
        bf16x4 hi = tr_read(sB + ks * 8192 + 1024 + b_off[nt]);
        bfr[nt] = __builtin_shufflevector(lo, hi, 0, 1, 2, 3, 4, 5, 6, 7);
      }
#pragma unroll
      for (int rt = 0; rt < 4; ++rt)
#pragma unroll
        for (int nt = 0; nt < 4; ++nt) {
          if (TYPE == 0)
            acc[rt][nt] = __builtin_amdgcn_mfma_f32_16x16x32_bf16(af[rt], bfr[nt], acc[rt][nt], 0, 0, 0);
          else
            acc[nt][rt] = __builtin_amdgcn_mfma_f32_16x16x32_bf16(bfr[nt], af[rt], acc[nt][rt], 0, 0, 0);
        }
    }
  }

  if (TYPE == 1) {
#pragma unroll
    for (int nt = 0; nt < 4; ++nt)
#pragma unroll
      for (int rt = 0; rt < 4; ++rt) {
        int m = m0 + wr * 64 + rt * 16 + l15;
        int n = 128 * j + (4 * wc + nt) * 16 + 4 * g;
        float v0 = phi_f(rbf(acc[nt][rt][0])), v1 = phi_f(rbf(acc[nt][rt][1]));
        float v2 = phi_f(rbf(acc[nt][rt][2])), v3 = phi_f(rbf(acc[nt][rt][3]));
        u32x2 o = {pack2(v0, v1), pack2(v2, v3)};
        *(u32x2*)(pq + (size_t)m * D_MODEL + n) = o;
      }
  } else if (TYPE == 2) {
#pragma unroll
    for (int pi = 0; pi < 2; ++pi)
#pragma unroll
      for (int rt = 0; rt < 4; ++rt) {
        int m = m0 + wr * 64 + rt * 16 + l15;
        int n = 16 * (4 * j + 2 * wc + pi) + 4 * g;
        float hv[4];
#pragma unroll
        for (int r = 0; r < 4; ++r) {
          float gate = rbf(acc[pi][rt][r]);
          float up = rbf(acc[pi + 2][rt][r]);
          float ge = rbf(gelu_f(gate));
          hv[r] = ge * up;
        }
        u32x2 o = {pack2(hv[0], hv[1]), pack2(hv[2], hv[3])};
        *(u32x2*)(hbuf + (size_t)m * F_DIM + n) = o;
      }
  } else {
    f32x4 kvacc[2], zacc[2];
    const bf16x8 ones = {0x3F80, 0x3F80, 0x3F80, 0x3F80, 0x3F80, 0x3F80, 0x3F80, 0x3F80};
#pragma unroll
    for (int a = 0; a < 2; ++a) {
      kvacc[a] = (f32x4){0.f, 0.f, 0.f, 0.f};
      zacc[a] = (f32x4){0.f, 0.f, 0.f, 0.f};
#pragma unroll
      for (int rp = 0; rp < 2; ++rp) {
        bf16x8 pkf, vf;
#pragma unroll
        for (int hh = 0; hh < 2; ++hh) {
          int rt = 2 * rp + hh;
#pragma unroll
          for (int r = 0; r < 4; ++r) {
            float pk = phi_f(rbf(acc[rt][a][r]));
            float vv = acc[rt][a + 2][r];
            pkf[hh * 4 + r] = (short)f2bf(pk);
            vf[hh * 4 + r] = (short)f2bf(vv);
          }
        }
        kvacc[a] = __builtin_amdgcn_mfma_f32_16x16x32_bf16(pkf, vf, kvacc[a], 0, 0, 0);
        zacc[a] = __builtin_amdgcn_mfma_f32_16x16x32_bf16(pkf, ones, zacc[a], 0, 0, 0);
      }
    }
    __syncthreads();
    float* red = (float*)smem;
    if (wr == 1) {
#pragma unroll
      for (int a = 0; a < 2; ++a) {
        float* rp = red + (wc * 2 + a) * 272;
#pragma unroll
        for (int r = 0; r < 4; ++r) rp[(4 * g + r) * 16 + l15] = kvacc[a][r];
        if (l15 == 0) {
#pragma unroll
          for (int r = 0; r < 4; ++r) rp[256 + 4 * g + r] = zacc[a][r];
        }
      }
    }
    __syncthreads();
    if (wr == 0) {
#pragma unroll
      for (int a = 0; a < 2; ++a) {
        int head = 4 * j + 2 * wc + a;
        float* rp = red + (wc * 2 + a) * 272;
        float* gp = kvp + ((size_t)mt * NHEAD + head) * 272;
#pragma unroll
        for (int r = 0; r < 4; ++r) gp[(4 * g + r) * 16 + l15] = kvacc[a][r] + rp[(4 * g + r) * 16 + l15];
        if (l15 == 0) {
#pragma unroll
          for (int r = 0; r < 4; ++r) gp[256 + 4 * g + r] = zacc[a][r] + rp[256 + 4 * g + r];
        }
      }
    }
  }
}

__global__ __launch_bounds__(256) void g1_kernel(const u16* __restrict__ xn, const u16* __restrict__ wqkv,
                                                 const u16* __restrict__ wg, const u16* __restrict__ wu,
                                                 u16* __restrict__ pq, u16* __restrict__ hbuf,
                                                 float* __restrict__ kvp) {
  __shared__ __attribute__((aligned(16))) char smem[65536];
  const int b = blockIdx.x;
  const int xcd = b & 7, idx = b >> 3;
  const int nt = xcd * 5 + (idx % 5);
  const int mt = idx / 5;
  if (nt < 8) g1_body<0>(xn, wqkv, wg, wu, pq, hbuf, kvp, smem, mt, nt);
  else if (nt < 12) g1_body<1>(xn, wqkv, wg, wu, pq, hbuf, kvp, smem, mt, nt - 8);
  else g1_body<2>(xn, wqkv, wg, wu, pq, hbuf, kvp, smem, mt, nt - 12);
}

__global__ __launch_bounds__(320) void kvred_kernel(const float* __restrict__ kvp, u16* __restrict__ kvT,
                                                    u16* __restrict__ zb) {
  const int h = blockIdx.x, i = threadIdx.x;
  if (i >= 272) return;
  float s = 0.f;
  for (int mt = 0; mt < G1_MT; ++mt) s += kvp[((size_t)mt * NHEAD + h) * 272 + i];
  if (i < 256) {
    int d = i >> 4, e = i & 15;
    kvT[h * 256 + e * 16 + d] = f2bf(s);
  } else {
    zb[h * 16 + (i - 256)] = f2bf(s);
  }
}

#define G2_BK 64
__global__ __launch_bounds__(256) void g2_kernel(const u16* __restrict__ x, const u16* __restrict__ pq,
                                                 const u16* __restrict__ hbuf, const u16* __restrict__ kvT,
                                                 const u16* __restrict__ zb, const u16* __restrict__ wo,
                                                 const u16* __restrict__ wd, u16* __restrict__ out) {
  __shared__ __attribute__((aligned(16))) char smem[65536 + 32768];
  char* sO = smem;
  char* sA0 = smem + 65536;
  char* sB0 = smem + 65536 + 16384;
  const int b = blockIdx.x;
  const int xcd = b & 7, idx = b >> 3;
  const int mt = xcd * 4 + (idx >> 3);
  const int ntile = idx & 7;
  const int m0 = mt * 64, n0 = ntile * 64;
  const int tid = threadIdx.x, w = tid >> 6, l = tid & 63;
  const int g = l >> 4, l15 = l & 15;
  const int wr = w >> 1, wc = w & 1;

  const u16* aptr[2];
  const u16* bptr_o[2];
  const u16* bptr_d[2];
#pragma unroll
  for (int t = 0; t < 2; ++t) {
    int P = 2 * w + t;
    int row = 8 * P + (l >> 3);
    int chunk = (l & 7) ^ ((row >> 1) & 7);
    aptr[t] = hbuf + (size_t)(m0 + row) * F_DIM + chunk * 8;
    int krow = 8 * P + (l >> 3);
    int fb = ((krow & 3) >> 1) | (((krow >> 3) & 1) << 1);
    int c = (l & 7) ^ (fb << 1);
    bptr_o[t] = wo + (size_t)krow * D_MODEL + n0 + c * 8;
    bptr_d[t] = wd + (size_t)krow * D_MODEL + n0 + c * 8;
  }
  auto stageB_o = [&](int kt, int buf) {
    char* sB = sB0 + buf * 8192;
#pragma unroll
    for (int t = 0; t < 2; ++t) {
      int P = 2 * w + t;
      __builtin_amdgcn_global_load_lds((const unsigned*)(bptr_o[t] + (size_t)kt * G2_BK * D_MODEL),
                                       (__attribute__((address_space(3))) unsigned*)(sB + P * 1024 + l * 16), 16, 0, 0);
    }
  };
  auto stage_d = [&](int kt, int buf) {
    char* sA = sA0 + buf * 8192;
    char* sB = sB0 + buf * 8192;
#pragma unroll
    for (int t = 0; t < 2; ++t) {
      int P = 2 * w + t;
      __builtin_amdgcn_global_load_lds((const unsigned*)(aptr[t] + kt * G2_BK),
                                       (__attribute__((address_space(3))) unsigned*)(sA + P * 1024 + l * 16), 16, 0, 0);
      __builtin_amdgcn_global_load_lds((const unsigned*)(bptr_d[t] + (size_t)kt * G2_BK * D_MODEL),
                                       (__attribute__((address_space(3))) unsigned*)(sB + P * 1024 + l * 16), 16, 0, 0);
    }
  };

  stageB_o(0, 0);

  {
    const int rt = w;
    const int mrow = m0 + 16 * rt + l15;
    const int lrow = 16 * rt + l15;
    for (int h = 0; h < NHEAD; ++h) {
      bf16x4 pqf = *(const bf16x4*)(pq + (size_t)mrow * D_MODEL + 16 * h + 4 * g);
      bf16x4 kvf = *(const bf16x4*)(kvT + h * 256 + l15 * 16 + 4 * g);
      bf16x4 zf = *(const bf16x4*)(zb + h * 16 + 4 * g);
      f32x4 num = {0.f, 0.f, 0.f, 0.f}, den = {0.f, 0.f, 0.f, 0.f};
      num = __builtin_amdgcn_mfma_f32_16x16x16bf16_1k(kvf, pqf, num, 0, 0, 0);
      den = __builtin_amdgcn_mfma_f32_16x16x16bf16_1k(zf, pqf, den, 0, 0, 0);
      float dn = rbf(rbf(den[0]) + 1e-6f);
      float o0 = rbf(num[0]) / dn, o1 = rbf(num[1]) / dn, o2 = rbf(num[2]) / dn, o3 = rbf(num[3]) / dn;
      u32x2 ov = {pack2(o0, o1), pack2(o2, o3)};
      int kstep = h >> 2;
      int chunk = (h & 3) * 2 + (g >> 1);
      char* dst = sO + kstep * 8192 + lrow * 128 + ((chunk ^ ((lrow >> 1) & 7)) * 16) + (g & 1) * 8;
      *(u32x2*)dst = ov;
    }
  }

  const int a_off0 = (wr * 32 + l15) * 128 + ((g ^ (l15 >> 1)) * 16);
  const int q = l15 >> 2, p = l & 3;
  const int fB = (q >> 1) | ((g & 1) << 1);
  int b_off[2];
#pragma unroll
  for (int nt = 0; nt < 2; ++nt) {
    int c = 4 * wc + 2 * nt + (p >> 1);
    b_off[nt] = (8 * g + q) * 128 + ((c ^ (fB << 1)) * 16) + (p & 1) * 8;
  }

  f32x4 accs[2][2], accb[2][2];
#pragma unroll
  for (int a = 0; a < 2; ++a)
#pragma unroll
    for (int c = 0; c < 2; ++c) { accs[a][c] = (f32x4){0.f, 0.f, 0.f, 0.f}; accb[a][c] = (f32x4){0.f, 0.f, 0.f, 0.f}; }

  for (int kt = 0; kt < 8; ++kt) {
    __syncthreads();
    if (kt + 1 < 8) stageB_o(kt + 1, (kt + 1) & 1);
    else stage_d(0, 0);
    const char* sA = sO + kt * 8192;
    const char* sB = sB0 + (kt & 1) * 8192;
#pragma unroll
    for (int ks = 0; ks < 2; ++ks) {
      bf16x8 af[2], bfr[2];
#pragma unroll
      for (int rt = 0; rt < 2; ++rt) af[rt] = *(const bf16x8*)(sA + rt * 2048 + (a_off0 ^ (ks * 64)));
#pragma unroll
      for (int nt = 0; nt < 2; ++nt) {
        bf16x4 lo = tr_read(sB + ks * 4096 + b_off[nt]);
        bf16x4 hi = tr_read(sB + ks * 4096 + 512 + b_off[nt]);
        bfr[nt] = __builtin_shufflevector(lo, hi, 0, 1, 2, 3, 4, 5, 6, 7);
      }
#pragma unroll
      for (int rt = 0; rt < 2; ++rt)
#pragma unroll
        for (int nt = 0; nt < 2; ++nt)
          accs[nt][rt] = __builtin_amdgcn_mfma_f32_16x16x32_bf16(bfr[nt], af[rt], accs[nt][rt], 0, 0, 0);
    }
  }
  const int NKD = F_DIM / G2_BK;
  for (int kt = 0; kt < NKD; ++kt) {
    __syncthreads();
    if (kt + 1 < NKD) stage_d(kt + 1, (kt + 1) & 1);
    const char* sA = sA0 + (kt & 1) * 8192;
    const char* sB = sB0 + (kt & 1) * 8192;
#pragma unroll
    for (int ks = 0; ks < 2; ++ks) {
      bf16x8 af[2], bfr[2];
#pragma unroll
      for (int rt = 0; rt < 2; ++rt) af[rt] = *(const bf16x8*)(sA + rt * 2048 + (a_off0 ^ (ks * 64)));
#pragma unroll
      for (int nt = 0; nt < 2; ++nt) {
        bf16x4 lo = tr_read(sB + ks * 4096 + b_off[nt]);
        bf16x4 hi = tr_read(sB + ks * 4096 + 512 + b_off[nt]);
        bfr[nt] = __builtin_shufflevector(lo, hi, 0, 1, 2, 3, 4, 5, 6, 7);
      }
#pragma unroll
      for (int rt = 0; rt < 2; ++rt)
#pragma unroll
        for (int nt = 0; nt < 2; ++nt)
          accb[nt][rt] = __builtin_amdgcn_mfma_f32_16x16x32_bf16(bfr[nt], af[rt], accb[nt][rt], 0, 0, 0);
    }
  }
#pragma unroll
  for (int nt = 0; nt < 2; ++nt)
#pragma unroll
    for (int rt = 0; rt < 2; ++rt) {
      int m = m0 + wr * 32 + rt * 16 + l15;
      int n = n0 + wc * 32 + nt * 16 + 4 * g;
      u32x2 xv = *(const u32x2*)(x + (size_t)m * D_MODEL + n);
      float res[4];
#pragma unroll
      for (int r = 0; r < 4; ++r) {
        float s = rbf(accs[nt][rt][r]);
        float bb = rbf(accb[nt][rt][r]);
        float gg = rbf(silu_f(bb));
        float t = rbf(s * gg);
        unsigned xw = xv[r >> 1];
        float xf = (r & 1) ? __uint_as_float(xw & 0xffff0000u) : __uint_as_float(xw << 16);
        res[r] = xf + t;
      }
      u32x2 o = {pack2(res[0], res[1]), pack2(res[2], res[3])};
      *(u32x2*)(out + (size_t)m * D_MODEL + n) = o;
    }
}

extern "C" void kernel_launch(void* const* d_in, const int* in_sizes, int n_in, void* d_out, int out_size,
                              void* d_ws, size_t ws_size, hipStream_t stream) {
  const u16* x = (const u16*)d_in[0];
  const u16* wqkv = (const u16*)d_in[1];
  const u16* wo = (const u16*)d_in[2];
  const u16* wg = (const u16*)d_in[4];
  const u16* wu = (const u16*)d_in[5];
  const u16* wd = (const u16*)d_in[6];
  u16* out = (u16*)d_out;
  char* ws = (char*)d_ws;
  u16* xn = (u16*)(ws + WS_XN);
  u16* pq = (u16*)(ws + WS_PQ);
  u16* hbuf = (u16*)(ws + WS_H);
  float* kvp = (float*)(ws + WS_KVP);
  u16* kvT = (u16*)(ws + WS_KVT);
  u16* zb = (u16*)(ws + WS_ZB);

  ln_kernel<<<S_LEN / 4, 256, 0, stream>>>(x, xn);
  g1_kernel<<<G1_NT * G1_MT, 256, 0, stream>>>(xn, wqkv, wg, wu, pq, hbuf, kvp);
  kvred_kernel<<<NHEAD, 320, 0, stream>>>(kvp, kvT, zb);
  g2_kernel<<<256, 256, 0, stream>>>(x, pq, hbuf, kvT, zb, wo, wd, out);
}
```

```cpp
#include <hip/hip_runtime.h>
#include <hip/hip_bf16.h>
#include <stdint.h>

typedef unsigned short u16;
typedef __attribute__((ext_vector_type(8))) short bf16x8;
typedef __attribute__((ext_vector_type(4))) short bf16x4;
typedef __attribute__((ext_vector_type(4))) float f32x4;
typedef __attribute__((ext_vector_type(2))) unsigned u32x2;
typedef __attribute__((ext_vector_type(4))) unsigned u32x4;
typedef __attribute__((address_space(3))) unsigned lds_u32;

#define S_LEN 2048
#define D_MODEL 512
#define F_DIM 1792
#define NHEAD 32

#define WS_XN      0
#define WS_PQ      (2u*1024*1024)
#define WS_H       (4u*1024*1024)
#define WS_KVP     (12u*1024*1024)
#define WS_KVT     (13u*1024*1024)
#define WS_ZB      (13u*1024*1024 + 65536)
#define WS_CNT     (14u*1024*1024)

__device__ __forceinline__ float bf2f(u16 v) { return __uint_as_float(((unsigned)v) << 16); }
__device__ __forceinline__ u16 f2bf(float f) {
  __bf16 b = (__bf16)f;
  return __builtin_bit_cast(u16, b);
}
__device__ __forceinline__ float rbf(float f) { return bf2f(f2bf(f)); }
__device__ __forceinline__ unsigned pack2(float lo, float hi) {
  return (unsigned)f2bf(lo) | ((unsigned)f2bf(hi) << 16);
}
__device__ __forceinline__ float phi_f(float t) {
  float r = (t > 0.f) ? rbf(t + 1.0f) : rbf(__expf(t));
  return rbf(r + 1e-6f);
}
__device__ __forceinline__ float gelu_f(float x) {
  const float k0 = 0.7978845608028654f, k1 = 0.044715f;
  float u = k0 * (x + k1 * x * x * x);
  float e = __expf(-2.0f * u);
  return x * __frcp_rn(1.0f + e);
}
__device__ __forceinline__ float silu_f(float x) { return x * __frcp_rn(1.0f + __expf(-x)); }

__device__ __forceinline__ float wave_sum(float v) {
#pragma unroll
  for (int o = 32; o > 0; o >>= 1) v += __shfl_xor(v, o, 64);
  return v;
}

#define WAIT_VM(n) asm volatile("s_waitcnt vmcnt(" #n ")" ::: "memory")
template <int N> __device__ __forceinline__ void wait_vm() {
  static_assert(N >= 0 && N < 64, "vmcnt range");
  asm volatile("s_waitcnt vmcnt(%0)" ::"n"(N) : "memory");
}
__device__ __forceinline__ void raw_barrier() {
  asm volatile("s_waitcnt lgkmcnt(0)" ::: "memory");
  __builtin_amdgcn_s_barrier();
  asm volatile("" ::: "memory");
}

__global__ __launch_bounds__(256) void ln_kernel(const u16* __restrict__ x, u16* __restrict__ xn,
                                                 unsigned* __restrict__ cnt) {
  const int wave = threadIdx.x >> 6, lane = threadIdx.x & 63;
  const int row = blockIdx.x * 4 + wave;
  if (blockIdx.x == 0 && threadIdx.x < 32) cnt[threadIdx.x * 64] = 0u;
  const u32x4 v = *(const u32x4*)(x + (size_t)row * D_MODEL + lane * 8);
  float f[8];
#pragma unroll
  for (int i = 0; i < 4; ++i) {
    f[2 * i] = __uint_as_float(v[i] << 16);
    f[2 * i + 1] = __uint_as_float(v[i] & 0xffff0000u);
  }
  float s = 0.f;
#pragma unroll
  for (int i = 0; i < 8; ++i) s += f[i];
  s = wave_sum(s);
  const float mean = s * (1.0f / D_MODEL);
  float ss = 0.f;
#pragma unroll
  for (int i = 0; i < 8; ++i) { float d = f[i] - mean; ss += d * d; }
  ss = wave_sum(ss);
  const float var = ss * (1.0f / D_MODEL);
  const float mb = rbf(mean);
  const float vb = rbf(var);
  const float den = rbf(sqrtf(rbf(vb + 1e-5f)));
  u32x4 o;
#pragma unroll
  for (int i = 0; i < 4; ++i) {
    float a = rbf(f[2 * i] - mb) / den;
    float b = rbf(f[2 * i + 1] - mb) / den;
    o[i] = pack2(a, b);
  }
  *(u32x4*)(xn + (size_t)row * D_MODEL + lane * 8) = o;
}

#define G1_NS 3
#define G1_NK 8
#define G1_STG 53248
#define G1_BOFF 32768

__device__ __forceinline__ bf16x4 tr_read(const char* lds_ptr) {
  return __builtin_amdgcn_ds_read_tr16_b64_v4i16((__attribute__((address_space(3))) bf16x4*)lds_ptr);
}

__device__ __forceinline__ void g1_body(const u16* __restrict__ xn, const u16* __restrict__ wqkv,
                                        const u16* __restrict__ wg, const u16* __restrict__ wu,
                                        u16* __restrict__ pqF, u16* __restrict__ hbuf,
                                        float* __restrict__ kvp, u16* __restrict__ kvT, u16* __restrict__ zb,
                                        unsigned* __restrict__ cnt, char* smem) {
  const int b = blockIdx.x;
  const int xcd = b & 7, idx = b >> 3;
  const int nt = xcd * 4 + (idx & 3);
  const int mt = idx >> 2;
  const int m0 = mt * 256;
  const int tid = threadIdx.x, w = tid >> 6, l = tid & 63;
  const int g = l >> 4, l15 = l & 15;

  if (w >= 8) {
    const int lw = w - 8;
    const int off_e = (l >> 3) * D_MODEL + (((l & 7) ^ (l >> 4)) * 8);
    const int off_o = (l >> 3) * D_MODEL + (((l & 7) ^ (4 + (l >> 4))) * 8);
    const u16* abase = xn + (size_t)(m0 + 64 * lw) * D_MODEL;
    const u16* bptr[5];
    int bld[5];
#pragma unroll
    for (int t = 0; t < 5; ++t) {
      int P = 5 * lw + t;
      int off = P * 1024 + l * 16;
      int row = off / 320, chunkp = (off % 320) >> 4;
      int chunk = chunkp ^ (((row >> 3) & 1) << 1);
      int u = chunk >> 1, half = chunk & 1;
      int pp = u % 5, member = u / 5;
      int PG = 5 * nt + pp;
      const u16* base;
      int ld;
      if (PG < 16) { base = wqkv + 32 * PG + 16 * member; ld = 1536; }
      else if (PG < 48) { base = wqkv + 512 + 512 * member + 16 * (PG - 16); ld = 1536; }
      else { base = (member ? wu : wg) + 16 * (PG - 48); ld = F_DIM; }
      bptr[t] = base + (size_t)row * ld + half * 8;
      bld[t] = 64 * ld;
    }
    auto stage = [&](int kt) {
      char* sA = smem + (kt % G1_NS) * G1_STG;
      char* sB = sA + G1_BOFF;
#pragma unroll
      for (int t = 0; t < 8; ++t) {
        const u16* src = abase + (size_t)(8 * t) * D_MODEL + kt * 64 + ((t & 1) ? off_o : off_e);
        __builtin_amdgcn_global_load_lds((const unsigned*)src, (lds_u32*)(sA + (8 * lw + t) * 1024 + l * 16), 16, 0, 0);
      }
#pragma unroll
      for (int t = 0; t < 5; ++t)
        __builtin_amdgcn_global_load_lds((const unsigned*)(bptr[t] + (size_t)kt * bld[t]),
                                         (lds_u32*)(sB + (5 * lw + t) * 1024 + l * 16), 16, 0, 0);
    };
    stage(0);
    stage(1);
    WAIT_VM(13);
    raw_barrier();
#pragma unroll
    for (int kt = 0; kt < G1_NK; ++kt) {
      if (kt + 2 < G1_NK) { stage(kt + 2); WAIT_VM(13); }
      else WAIT_VM(0);
      if (kt + 1 < G1_NK) raw_barrier();
    }
    return;
  }

  const int cw = w;
  const int a_off0 = (32 * cw + l15) * 128 + ((g ^ (l15 >> 1)) * 16);
  const int q = l15 >> 2, p4 = l & 3;
  const int bb = g & 1;
  const int base_b = G1_BOFF + (8 * g + q) * 320 + (p4 >> 1) * 16 + (p4 & 1) * 8;
  const int baseE = base_b + bb * 32, baseO = base_b + (1 - bb) * 32;

  f32x4 acc[10][2];
#pragma unroll
  for (int u = 0; u < 10; ++u)
#pragma unroll
    for (int rt = 0; rt < 2; ++rt) acc[u][rt] = (f32x4){0.f, 0.f, 0.f, 0.f};

  raw_barrier();
  for (int kt = 0; kt < G1_NK; ++kt) {
    const char* sS = smem + (kt % G1_NS) * G1_STG;
#pragma unroll
    for (int ks = 0; ks < 2; ++ks) {
      bf16x8 af[2];
#pragma unroll
      for (int rt = 0; rt < 2; ++rt) af[rt] = *(const bf16x8*)(sS + rt * 2048 + (a_off0 ^ (ks * 64)));
#pragma unroll
      for (int u = 0; u < 10; ++u) {
        const char* bp = sS + ks * 10240 + ((u & 1) ? baseO : baseE) + (u >> 1) * 64;
        bf16x4 lo = tr_read(bp);
        bf16x4 hi = tr_read(bp + 1280);
        bf16x8 bf = __builtin_shufflevector(lo, hi, 0, 1, 2, 3, 4, 5, 6, 7);
#pragma unroll
        for (int rt = 0; rt < 2; ++rt)
          acc[u][rt] = __builtin_amdgcn_mfma_f32_16x16x32_bf16(bf, af[rt], acc[u][rt], 0, 0, 0);
      }
    }
    if (kt + 1 < G1_NK) raw_barrier();
  }

  bool has_kv = false;
#pragma unroll
  for (int pp = 0; pp < 5; ++pp) {
    const int PG = 5 * nt + pp;
    if (PG < 16) {
#pragma unroll
      for (int rt = 0; rt < 2; ++rt) {
        int rowt = (m0 + 32 * cw + 16 * rt) >> 4;
#pragma unroll
        for (int mem = 0; mem < 2; ++mem) {
          f32x4 a = acc[pp + 5 * mem][rt];
          float v0 = phi_f(rbf(a[0])), v1 = phi_f(rbf(a[1])), v2 = phi_f(rbf(a[2])), v3 = phi_f(rbf(a[3]));
          u32x2 o = {pack2(v0, v1), pack2(v2, v3)};
          *(u32x2*)(pqF + (((size_t)rowt * 16 + PG) * 64 + l) * 8 + mem * 4) = o;
        }
      }
    } else if (PG >= 48) {
      const int gi = PG - 48;
#pragma unroll
      for (int rt = 0; rt < 2; ++rt) {
        int m = m0 + 32 * cw + 16 * rt + l15;
        float hv[4];
#pragma unroll
        for (int r = 0; r < 4; ++r) {
          float gate = rbf(acc[pp][rt][r]);
          float up = rbf(acc[pp + 5][rt][r]);
          float ge = rbf(gelu_f(gate));
          hv[r] = ge * up;
        }
        u32x2 o = {pack2(hv[0], hv[1]), pack2(hv[2], hv[3])};
        *(u32x2*)(hbuf + (size_t)m * F_DIM + 16 * gi + 4 * g) = o;
      }
    } else {
      has_kv = true;
    }
  }
  if (has_kv) {
    __syncthreads();
    int tz = threadIdx.x;
    asm volatile("" : "+v"(tz));
    const int g = (tz & 63) >> 4, l15 = tz & 15, q = l15 >> 2, p4 = tz & 3;
    char* scr = smem + cw * 2048;
    float* red = (float*)(smem + 16384);
    const bf16x8 ones = {0x3F80, 0x3F80, 0x3F80, 0x3F80, 0x3F80, 0x3F80, 0x3F80, 0x3F80};
#pragma unroll
    for (int pp = 0; pp < 5; ++pp) {
      const int PG = 5 * nt + pp;
      if (PG >= 16 && PG < 48) {
#pragma unroll
        for (int rt = 0; rt < 2; ++rt) {
          f32x4 a = acc[pp][rt], v = acc[pp + 5][rt];
          float k0 = phi_f(rbf(a[0])), k1 = phi_f(rbf(a[1])), k2 = phi_f(rbf(a[2])), k3 = phi_f(rbf(a[3]));
          u32x2 pk = {pack2(k0, k1), pack2(k2, k3)};
          u32x2 vv = {pack2(v[0], v[1]), pack2(v[2], v[3])};
          *(u32x2*)(scr + (16 * rt + l15) * 32 + g * 8) = pk;
          *(u32x2*)(scr + 1024 + (16 * rt + l15) * 32 + g * 8) = vv;
        }
        asm volatile("s_waitcnt lgkmcnt(0)" ::: "memory");
        const char* rp = scr + (8 * g + q) * 32 + p4 * 8;
        bf16x4 a_lo = tr_read(rp), a_hi = tr_read(rp + 128);
        bf16x4 b_lo = tr_read(rp + 1024), b_hi = tr_read(rp + 1024 + 128);
        bf16x8 af = __builtin_shufflevector(a_lo, a_hi, 0, 1, 2, 3, 4, 5, 6, 7);
        bf16x8 bf = __builtin_shufflevector(b_lo, b_hi, 0, 1, 2, 3, 4, 5, 6, 7);
        f32x4 kvacc = {0.f, 0.f, 0.f, 0.f}, zacc = {0.f, 0.f, 0.f, 0.f};
        kvacc = __builtin_amdgcn_mfma_f32_16x16x32_bf16(af, bf, kvacc, 0, 0, 0);
        zacc = __builtin_amdgcn_mfma_f32_16x16x32_bf16(af, ones, zacc, 0, 0, 0);
        float* rr = red + (cw * 5 + pp) * 272;
        *(f32x4*)(rr + l15 * 16 + 4 * g) = kvacc;
        if (l15 == 0) *(f32x4*)(rr + 256 + 4 * g) = zacc;
        asm volatile("s_waitcnt lgkmcnt(0)" ::: "memory");
      }
    }
    __syncthreads();
    for (int i = tid; i < 5 * 272; i += 512) {
      int pp = i / 272, e = i % 272;
      int PG = 5 * nt + pp;
      if (PG >= 16 && PG < 48) {
        float s = 0.f;
#pragma unroll
        for (int c = 0; c < 8; ++c) s += red[(c * 5 + pp) * 272 + e];
        __hip_atomic_store(kvp + ((size_t)mt * NHEAD + (PG - 16)) * 272 + e, s, __ATOMIC_RELAXED, __HIP_MEMORY_SCOPE_AGENT);
      }
    }
    WAIT_VM(0);
    __syncthreads();
    unsigned* flag = (unsigned*)(smem + 65536);
    if (tid == 0) {
      unsigned old = __hip_atomic_fetch_add(cnt + nt * 64, 1u, __ATOMIC_RELAXED, __HIP_MEMORY_SCOPE_AGENT);
      *flag = old;
    }
    __syncthreads();
    if (*flag == 7u) {
      for (int i = tid; i < 5 * 272; i += 512) {
        int pp = i / 272, e = i % 272;
        int PG = 5 * nt + pp;
        if (PG >= 16 && PG < 48) {
          int head = PG - 16;
          const float* src = kvp + (size_t)head * 272 + e;
          float v[8];
#pragma unroll
          for (int m = 0; m < 8; ++m)
            v[m] = __hip_atomic_load(src + (size_t)m * NHEAD * 272, __ATOMIC_RELAXED, __HIP_MEMORY_SCOPE_AGENT);
          float s = 0.f;
#pragma unroll
          for (int m = 0; m < 8; ++m) s += v[m];
          if (e < 256) kvT[head * 256 + e] = f2bf(s);
          else zb[head * 16 + (e - 256)] = f2bf(s);
        }
      }
    }
  }
}

__global__ __launch_bounds__(768) void g1_kernel(const u16* __restrict__ xn, const u16* __restrict__ wqkv,
                                                 const u16* __restrict__ wg, const u16* __restrict__ wu,
                                                 u16* __restrict__ pqF, u16* __restrict__ hbuf,
                                                 float* __restrict__ kvp, u16* __restrict__ kvT,
                                                 u16* __restrict__ zb, unsigned* __restrict__ cnt) {
  __shared__ __attribute__((aligned(16))) char smem[G1_NS * G1_STG];
  g1_body(xn, wqkv, wg, wu, pqF, hbuf, kvp, kvT, zb, cnt, smem);
}

#define G2_NS 6
#define G2_NP 36
#define G2_STAGE 16384
#define G2_OSLOTS 3
#define G2_OFF_RING 0
#define G2_OFF_O    (G2_NS * G2_STAGE)
#define G2_OFF_KV   (G2_OFF_O + G2_OSLOTS * 8192)
#define G2_OFF_Z    (G2_OFF_KV + 16384)
#define G2_LDS      (G2_OFF_Z + 1024)

__host__ __device__ constexpr int g2_nB(int kt) {
  int v = ((4 * kt + 4) * 26 + 31) / 32 + 1;
  return v > 28 ? 28 : v;
}
__host__ __device__ constexpr int g2_posS(int kt) { return g2_nB(kt) + kt; }
__host__ __device__ constexpr int g2_sidx(int p) {
  for (int kt = 0; kt < 8; ++kt) if (g2_posS(kt) == p) return kt;
  return -1;
}
__host__ __device__ constexpr int g2_bidx(int p) {
  int c = 0;
  for (int kt = 0; kt < 8; ++kt) if (g2_posS(kt) < p) ++c;
  return p - c;
}
__host__ __device__ constexpr int g2_hb(int b) { int v = (b * 32) / 26; return v > 32 ? 32 : v; }
__host__ __device__ constexpr int g2_cnt(int p) { return (g2_sidx(p) >= 0) ? 2 : 4; }
__host__ __device__ constexpr int g2_allowed(int s) {
  int last = (s + G2_NS - 1 < G2_NP - 1) ? (s + G2_NS - 1) : (G2_NP - 1);
  int a = 0;
  for (int t = s + 3; t <= last; ++t) a += g2_cnt(t);
  return a;
}

__device__ __forceinline__ float div_nr(float a, float b, float rb) {
  float q = a * rb;
  float e = __builtin_fmaf(-q, b, a);
  return __builtin_fmaf(e, rb, q);
}

__device__ __forceinline__ void g2_body(const u16* __restrict__ x, const u16* __restrict__ pq,
                                        const u16* __restrict__ hbuf, const u16* __restrict__ kvT,
                                        const u16* __restrict__ zb, const u16* __restrict__ wo,
                                        const u16* __restrict__ wd, u16* __restrict__ out, char* smem) {
  char* sR = smem + G2_OFF_RING;
  char* sO = smem + G2_OFF_O;
  const int b = blockIdx.x;
  const int xcd = b & 7, idx = b >> 3;
  const int mt = xcd * 4 + (idx >> 3);
  const int ntile = idx & 7;
  const int m0 = mt * 64, n0 = ntile * 64;
  const int tid = threadIdx.x, w = tid >> 6, l = tid & 63;
  const int g = l >> 4, l15 = l & 15;

  if (w >= 4) {
    const int lw = w - 4;
    const u16* aptr[2];
    const u16* bptr_o[2];
    const u16* bptr_d[2];
#pragma unroll
    for (int t = 0; t < 2; ++t) {
      int P = 2 * lw + t;
      int row = 8 * P + (l >> 3);
      int chunk = (l & 7) ^ ((row >> 1) & 7);
      aptr[t] = hbuf + (size_t)(m0 + row) * F_DIM + chunk * 8;
      int krow = 8 * P + (l >> 3);
      int fb = ((krow >> 1) & 1) | (((krow >> 3) & 1) << 1);
      int c = (l & 7) ^ (fb << 1);
      bptr_o[t] = wo + (size_t)krow * D_MODEL + n0 + c * 8;
      bptr_d[t] = wd + (size_t)krow * D_MODEL + n0 + c * 8;
    }
#pragma unroll
    for (int t = 0; t < 4; ++t)
      __builtin_amdgcn_global_load_lds((const unsigned*)(kvT + (size_t)(4 * t + lw) * 512 + l * 8),
                                       (lds_u32*)(smem + G2_OFF_KV + (4 * t + lw) * 1024 + l * 16), 16, 0, 0);
    if (lw == 0)
      __builtin_amdgcn_global_load_lds((const unsigned*)(zb + l * 8), (lds_u32*)(smem + G2_OFF_Z + l * 16), 16, 0, 0);

    auto stage = [&](int p, int si, int bi) {
      char* sA = sR + (p % G2_NS) * G2_STAGE;
      char* sB = sA + 8192;
#pragma unroll
      for (int t = 0; t < 2; ++t) {
        int P = 2 * lw + t;
        if (si < 0) {
          __builtin_amdgcn_global_load_lds((const unsigned*)(aptr[t] + bi * 64), (lds_u32*)(sA + P * 1024 + l * 16), 16, 0, 0);
          __builtin_amdgcn_global_load_lds((const unsigned*)(bptr_d[t] + (size_t)bi * 64 * D_MODEL), (lds_u32*)(sB + P * 1024 + l * 16), 16, 0, 0);
        } else {
          __builtin_amdgcn_global_load_lds((const unsigned*)(bptr_o[t] + (size_t)si * 64 * D_MODEL), (lds_u32*)(sB + P * 1024 + l * 16), 16, 0, 0);
        }
      }
    };
#define G2_STAGE_AT(p) stage(p, g2_sidx(p), g2_bidx(p))
    G2_STAGE_AT(0); G2_STAGE_AT(1); G2_STAGE_AT(2); G2_STAGE_AT(3); G2_STAGE_AT(4);
    wait_vm<g2_allowed(-1)>();
    raw_barrier();
#define G2_LSTEP(s)                                                       \
    {                                                                     \
      if (s + G2_NS - 1 < G2_NP) G2_STAGE_AT(s + G2_NS - 1);              \
      if (s + 1 < G2_NP) { wait_vm<g2_allowed(s)>(); raw_barrier(); }     \
    }
    G2_LSTEP(0) G2_LSTEP(1) G2_LSTEP(2) G2_LSTEP(3) G2_LSTEP(4) G2_LSTEP(5) G2_LSTEP(6) G2_LSTEP(7)
    G2_LSTEP(8) G2_LSTEP(9) G2_LSTEP(10) G2_LSTEP(11) G2_LSTEP(12) G2_LSTEP(13) G2_LSTEP(14) G2_LSTEP(15)
    G2_LSTEP(16) G2_LSTEP(17) G2_LSTEP(18) G2_LSTEP(19) G2_LSTEP(20) G2_LSTEP(21) G2_LSTEP(22) G2_LSTEP(23)
    G2_LSTEP(24) G2_LSTEP(25) G2_LSTEP(26) G2_LSTEP(27) G2_LSTEP(28) G2_LSTEP(29) G2_LSTEP(30) G2_LSTEP(31)
    G2_LSTEP(32) G2_LSTEP(33) G2_LSTEP(34) G2_LSTEP(35)
    return;
  }
  const int wr = w >> 1, wc = w & 1;
  const int a_off0 = (wr * 32 + l15) * 128 + ((g ^ (l15 >> 1)) * 16);
  const int q = l15 >> 2, p4 = l & 3;
  const int fB = (q >> 1) | ((g & 1) << 1);
  int b_off[2];
#pragma unroll
  for (int nt = 0; nt < 2; ++nt) {
    int c = 4 * wc + 2 * nt + (p4 >> 1);
    b_off[nt] = 8192 + (8 * g + q) * 128 + ((c ^ (fB << 1)) * 16) + (p4 & 1) * 8;
  }
  const int lrowA = 16 * w + l15;
  const char* kv_l = smem + G2_OFF_KV + l15 * 32 + g * 8;
  const char* z_l = smem + G2_OFF_Z + g * 8;
  char* sO_l = sO + lrowA * 128 + (g & 1) * 8;
  const int swzA = (lrowA >> 1) & 7;
  bf16x8 pqf2[NHEAD / 2];
#pragma unroll
  for (int hp = 0; hp < NHEAD / 2; ++hp)
    pqf2[hp] = *(const bf16x8*)(pq + ((((size_t)(m0 >> 4) + w) * 16 + hp) * 64 + l) * 8);

  f32x4 accs[2][2], accb[2][2];
#pragma unroll
  for (int a = 0; a < 2; ++a)
#pragma unroll
    for (int c = 0; c < 2; ++c) { accs[a][c] = (f32x4){0.f, 0.f, 0.f, 0.f}; accb[a][c] = (f32x4){0.f, 0.f, 0.f, 0.f}; }

  bf16x8 af[2][2][2], bfr[2][2][2];
  auto read_frags = [&](int p, int si, int buf) {
    const char* sS = sR + (p % G2_NS) * G2_STAGE;
    const char* sAx = (si >= 0) ? (sO + (si % G2_OSLOTS) * 8192) : sS;
#pragma unroll
    for (int ks = 0; ks < 2; ++ks) {
#pragma unroll
      for (int rt = 0; rt < 2; ++rt) af[buf][ks][rt] = *(const bf16x8*)(sAx + rt * 2048 + (a_off0 ^ (ks * 64)));
#pragma unroll
      for (int nt = 0; nt < 2; ++nt) {
        bf16x4 lo = tr_read(sS + ks * 4096 + b_off[nt]);
        bf16x4 hi = tr_read(sS + ks * 4096 + 512 + b_off[nt]);
        bfr[buf][ks][nt] = __builtin_shufflevector(lo, hi, 0, 1, 2, 3, 4, 5, 6, 7);
      }
    }
  };
  u32x2 xv[2][2];

  raw_barrier();
  read_frags(0, g2_sidx(0), 0);

#define G2_CSTEP(s)                                                                                    \
  {                                                                                                    \
    constexpr int cb = (s) & 1;                                                                        \
    constexpr int si = g2_sidx(s), bi = g2_bidx(s);                                                    \
    if (s + 1 < G2_NP) read_frags(s + 1, g2_sidx(s + 1), cb ^ 1);                                      \
    _Pragma("unroll") for (int ks = 0; ks < 2; ++ks)                                                   \
    _Pragma("unroll") for (int rt = 0; rt < 2; ++rt)                                                   \
    _Pragma("unroll") for (int nt = 0; nt < 2; ++nt) {                                                 \
      if (si >= 0)                                                                                     \
        accs[nt][rt] = __builtin_amdgcn_mfma_f32_16x16x32_bf16(bfr[cb][ks][nt], af[cb][ks][rt], accs[nt][rt], 0, 0, 0); \
      else                                                                                             \
        accb[nt][rt] = __builtin_amdgcn_mfma_f32_16x16x32_bf16(bfr[cb][ks][nt], af[cb][ks][rt], accb[nt][rt], 0, 0, 0); \
    }                                                                                                  \
    if (si < 0) {                                                                                      \
      constexpr int h0 = g2_hb(bi), h1 = g2_hb(bi + 1);                                                \
      _Pragma("unroll") for (int h = h0; h < h1; ++h) {                                                \
        bf16x4 kvf = *(const bf16x4*)(kv_l + h * 512);                                                 \
        bf16x4 zf = *(const bf16x4*)(z_l + h * 32);                                                    \
        f32x4 num = {0.f, 0.f, 0.f, 0.f}, den = {0.f, 0.f, 0.f, 0.f};                                  \
        bf16x4 pqh = (h & 1) ? __builtin_shufflevector(pqf2[h >> 1], pqf2[h >> 1], 4, 5, 6, 7)          \
                             : __builtin_shufflevector(pqf2[h >> 1], pqf2[h >> 1], 0, 1, 2, 3);         \
        num = __builtin_amdgcn_mfma_f32_16x16x16bf16_1k(kvf, pqh, num, 0, 0, 0);                       \
        den = __builtin_amdgcn_mfma_f32_16x16x16bf16_1k(zf, pqh, den, 0, 0, 0);                        \
        float dn = rbf(rbf(den[0]) + 1e-6f);                                                           \
        float rd = __builtin_amdgcn_rcpf(dn);                                                          \
        float o0 = div_nr(rbf(num[0]), dn, rd), o1 = div_nr(rbf(num[1]), dn, rd);                      \
        float o2 = div_nr(rbf(num[2]), dn, rd), o3 = div_nr(rbf(num[3]), dn, rd);                      \
        u32x2 ov = {pack2(o0, o1), pack2(o2, o3)};                                                     \
        *(u32x2*)(sO_l + ((h >> 2) % G2_OSLOTS) * 8192 + ((((h & 3) * 2 + (g >> 1)) ^ swzA) * 16)) = ov; \
      }                                                                                                \
    }                                                                                                  \
    if (s == 30) {                                                                                     \
      _Pragma("unroll") for (int nt = 0; nt < 2; ++nt)                                                 \
      _Pragma("unroll") for (int rt = 0; rt < 2; ++rt)                                                 \
        xv[nt][rt] = *(const u32x2*)(x + (size_t)(m0 + wr * 32 + rt * 16 + l15) * D_MODEL + n0 + wc * 32 + nt * 16 + 4 * g); \
    }                                                                                                  \
    if (s + 1 < G2_NP) raw_barrier();                                                                  \
  }
  G2_CSTEP(0) G2_CSTEP(1) G2_CSTEP(2) G2_CSTEP(3) G2_CSTEP(4) G2_CSTEP(5) G2_CSTEP(6) G2_CSTEP(7)
  G2_CSTEP(8) G2_CSTEP(9) G2_CSTEP(10) G2_CSTEP(11) G2_CSTEP(12) G2_CSTEP(13) G2_CSTEP(14) G2_CSTEP(15)
  G2_CSTEP(16) G2_CSTEP(17) G2_CSTEP(18) G2_CSTEP(19) G2_CSTEP(20) G2_CSTEP(21) G2_CSTEP(22) G2_CSTEP(23)
  G2_CSTEP(24) G2_CSTEP(25) G2_CSTEP(26) G2_CSTEP(27) G2_CSTEP(28) G2_CSTEP(29) G2_CSTEP(30) G2_CSTEP(31)
  G2_CSTEP(32) G2_CSTEP(33) G2_CSTEP(34) G2_CSTEP(35)

#pragma unroll
  for (int nt = 0; nt < 2; ++nt)
#pragma unroll
    for (int rt = 0; rt < 2; ++rt) {
      int m = m0 + wr * 32 + rt * 16 + l15;
      int n = n0 + wc * 32 + nt * 16 + 4 * g;
      float res[4];
#pragma unroll
      for (int r = 0; r < 4; ++r) {
        float s = rbf(accs[nt][rt][r]);
        float bb = rbf(accb[nt][rt][r]);
        float gg = rbf(silu_f(bb));
        float t = rbf(s * gg);
        unsigned xw = xv[nt][rt][r >> 1];
        float xf = (r & 1) ? __uint_as_float(xw & 0xffff0000u) : __uint_as_float(xw << 16);
        res[r] = xf + t;
      }
      u32x2 o = {pack2(res[0], res[1]), pack2(res[2], res[3])};
      *(u32x2*)(out + (size_t)m * D_MODEL + n) = o;
    }
}

__global__ __launch_bounds__(512) void g2_kernel(const u16* __restrict__ x, const u16* __restrict__ pq,
                                                 const u16* __restrict__ hbuf, const u16* __restrict__ kvT,
                                                 const u16* __restrict__ zb, const u16* __restrict__ wo,
                                                 const u16* __restrict__ wd, u16* __restrict__ out) {
  __shared__ __attribute__((aligned(16))) char smem[G2_LDS];
  g2_body(x, pq, hbuf, kvT, zb, wo, wd, out, smem);
}

extern "C" void kernel_launch(void* const* d_in, const int* in_sizes, int n_in, void* d_out, int out_size,
                              void* d_ws, size_t ws_size, hipStream_t stream) {
  const u16* x = (const u16*)d_in[0];
  const u16* wqkv = (const u16*)d_in[1];
  const u16* wo = (const u16*)d_in[2];
  const u16* wg = (const u16*)d_in[4];
  const u16* wu = (const u16*)d_in[5];
  const u16* wd = (const u16*)d_in[6];
  u16* out = (u16*)d_out;
  char* ws = (char*)d_ws;
  u16* xn = (u16*)(ws + WS_XN);
  u16* pq = (u16*)(ws + WS_PQ);
  u16* hbuf = (u16*)(ws + WS_H);
  float* kvp = (float*)(ws + WS_KVP);
  u16* kvT = (u16*)(ws + WS_KVT);
  u16* zb = (u16*)(ws + WS_ZB);
  unsigned* cnt = (unsigned*)(ws + WS_CNT);

  ln_kernel<<<S_LEN / 4, 256, 0, stream>>>(x, xn, cnt);
  g1_kernel<<<256, 768, 0, stream>>>(xn, wqkv, wg, wu, pq, hbuf, kvp, kvT, zb, cnt);
  g2_kernel<<<256, 512, 0, stream>>>(x, pq, hbuf, kvT, zb, wo, wd, out);
}
```

```cpp
#include <hip/hip_runtime.h>
#include <hip/hip_bf16.h>
#include <stdint.h>

typedef unsigned short u16;
typedef __attribute__((ext_vector_type(8))) short bf16x8;
typedef __attribute__((ext_vector_type(4))) short bf16x4;
typedef __attribute__((ext_vector_type(4))) float f32x4;
typedef __attribute__((ext_vector_type(2))) unsigned u32x2;
typedef __attribute__((ext_vector_type(4))) unsigned u32x4;
typedef __attribute__((address_space(3))) unsigned lds_u32;

#define S_LEN 2048
#define D_MODEL 512
#define F_DIM 1792
#define NHEAD 32

#define WS_XN      0
#define WS_PQ      (2u*1024*1024)
#define WS_H       (4u*1024*1024)
#define WS_KVP     (12u*1024*1024)
#define WS_KVT     (13u*1024*1024)
#define WS_ZB      (13u*1024*1024 + 65536)
#define WS_CNT     (14u*1024*1024)

__device__ __forceinline__ float bf2f(u16 v) { return __uint_as_float(((unsigned)v) << 16); }
__device__ __forceinline__ u16 f2bf(float f) {
  __bf16 b = (__bf16)f;
  return __builtin_bit_cast(u16, b);
}
__device__ __forceinline__ float rbf(float f) { return bf2f(f2bf(f)); }
__device__ __forceinline__ unsigned pack2(float lo, float hi) {
  return (unsigned)f2bf(lo) | ((unsigned)f2bf(hi) << 16);
}
__device__ __forceinline__ float phi_f(float t) {
  float r = (t > 0.f) ? rbf(t + 1.0f) : rbf(__expf(t));
  return rbf(r + 1e-6f);
}
__device__ __forceinline__ float gelu_f(float x) {
  const float k0 = 0.7978845608028654f, k1 = 0.044715f;
  float u = k0 * (x + k1 * x * x * x);
  float e = __expf(-2.0f * u);
  return x * __frcp_rn(1.0f + e);
}
__device__ __forceinline__ float silu_f(float x) { return x * __frcp_rn(1.0f + __expf(-x)); }

__device__ __forceinline__ float wave_sum(float v) {
#pragma unroll
  for (int o = 32; o > 0; o >>= 1) v += __shfl_xor(v, o, 64);
  return v;
}

#define WAIT_VM(n) asm volatile("s_waitcnt vmcnt(" #n ")" ::: "memory")
template <int N> __device__ __forceinline__ void wait_vm() {
  static_assert(N >= 0 && N < 64, "vmcnt range");
  asm volatile("s_waitcnt vmcnt(%0)" ::"n"(N) : "memory");
}
__device__ __forceinline__ void raw_barrier() {
  asm volatile("s_waitcnt lgkmcnt(0)" ::: "memory");
  __builtin_amdgcn_s_barrier();
  asm volatile("" ::: "memory");
}

__global__ __launch_bounds__(256) void ln_kernel(const u16* __restrict__ x, u16* __restrict__ xn,
                                                 unsigned* __restrict__ cnt) {
  const int wave = threadIdx.x >> 6, lane = threadIdx.x & 63;
  const int row = blockIdx.x * 4 + wave;
  if (blockIdx.x == 0 && threadIdx.x < 32) cnt[threadIdx.x * 64] = 0u;
  const u32x4 v = *(const u32x4*)(x + (size_t)row * D_MODEL + lane * 8);
  float f[8];
#pragma unroll
  for (int i = 0; i < 4; ++i) {
    f[2 * i] = __uint_as_float(v[i] << 16);
    f[2 * i + 1] = __uint_as_float(v[i] & 0xffff0000u);
  }
  float s = 0.f;
#pragma unroll
  for (int i = 0; i < 8; ++i) s += f[i];
  s = wave_sum(s);
  const float mean = s * (1.0f / D_MODEL);
  float ss = 0.f;
#pragma unroll
  for (int i = 0; i < 8; ++i) { float d = f[i] - mean; ss += d * d; }
  ss = wave_sum(ss);
  const float var = ss * (1.0f / D_MODEL);
  const float mb = rbf(mean);
  const float vb = rbf(var);
  const float den = rbf(sqrtf(rbf(vb + 1e-5f)));
  u32x4 o;
#pragma unroll
  for (int i = 0; i < 4; ++i) {
    float a = rbf(f[2 * i] - mb) / den;
    float b = rbf(f[2 * i + 1] - mb) / den;
    o[i] = pack2(a, b);
  }
  *(u32x4*)(xn + (size_t)row * D_MODEL + lane * 8) = o;
}

#define G1_NS 3
#define G1_NK 8
#define G1_STG 53248
#define G1_BOFF 32768

__device__ __forceinline__ bf16x4 tr_read(const char* lds_ptr) {
  return __builtin_amdgcn_ds_read_tr16_b64_v4i16((__attribute__((address_space(3))) bf16x4*)lds_ptr);
}

__device__ __forceinline__ void g1_body(const u16* __restrict__ xn, const u16* __restrict__ wqkv,
                                        const u16* __restrict__ wg, const u16* __restrict__ wu,
                                        u16* __restrict__ pqF, u16* __restrict__ hbuf,
                                        float* __restrict__ kvp, u16* __restrict__ kvT, u16* __restrict__ zb,
                                        unsigned* __restrict__ cnt, char* smem) {
  const int b = blockIdx.x;
  const int xcd = b & 7, idx = b >> 3;
  const int nt = xcd * 4 + (idx & 3);
  const int mt = idx >> 2;
  const int m0 = mt * 256;
  const int tid = threadIdx.x, w = tid >> 6, l = tid & 63;
  const int g = l >> 4, l15 = l & 15;

  if (w >= 8) {
    const int lw = w - 8;
    const int off_e = (l >> 3) * D_MODEL + (((l & 7) ^ (l >> 4)) * 8);
    const int off_o = (l >> 3) * D_MODEL + (((l & 7) ^ (4 + (l >> 4))) * 8);
    const u16* abase = xn + (size_t)(m0 + 64 * lw) * D_MODEL;
    const u16* bptr[5];
    int bld[5];
#pragma unroll
    for (int t = 0; t < 5; ++t) {
      int P = 5 * lw + t;
      int off = P * 1024 + l * 16;
      int row = off / 320, chunkp = (off % 320) >> 4;
      int chunk = chunkp ^ (((row >> 3) & 1) << 1);
      int u = chunk >> 1, half = chunk & 1;
      int pp = u % 5, member = u / 5;
      int PG = 5 * nt + pp;
      const u16* base;
      int ld;
      if (PG < 16) { base = wqkv + 32 * PG + 16 * member; ld = 1536; }
      else if (PG < 48) { base = wqkv + 512 + 512 * member + 16 * (PG - 16); ld = 1536; }
      else { base = (member ? wu : wg) + 16 * (PG - 48); ld = F_DIM; }
      bptr[t] = base + (size_t)row * ld + half * 8;
      bld[t] = 64 * ld;
    }
    auto stage = [&](int kt) {
      char* sA = smem + (kt % G1_NS) * G1_STG;
      char* sB = sA + G1_BOFF;
#pragma unroll
      for (int t = 0; t < 8; ++t) {
        const u16* src = abase + (size_t)(8 * t) * D_MODEL + kt * 64 + ((t & 1) ? off_o : off_e);
        __builtin_amdgcn_global_load_lds((const unsigned*)src, (lds_u32*)(sA + (8 * lw + t) * 1024 + l * 16), 16, 0, 0);
      }
#pragma unroll
      for (int t = 0; t < 5; ++t)
        __builtin_amdgcn_global_load_lds((const unsigned*)(bptr[t] + (size_t)kt * bld[t]),
                                         (lds_u32*)(sB + (5 * lw + t) * 1024 + l * 16), 16, 0, 0);
    };
    stage(0);
    stage(1);
    WAIT_VM(13);
    raw_barrier();
#pragma unroll
    for (int kt = 0; kt < G1_NK; ++kt) {
      if (kt + 2 < G1_NK) { stage(kt + 2); WAIT_VM(13); }
      else WAIT_VM(0);
      if (kt + 1 < G1_NK) raw_barrier();
    }
    return;
  }

  const int cw = w;
  const int a_off0 = (32 * cw + l15) * 128 + ((g ^ (l15 >> 1)) * 16);
  const int q = l15 >> 2, p4 = l & 3;
  const int bb = g & 1;
  const int base_b = G1_BOFF + (8 * g + q) * 320 + (p4 >> 1) * 16 + (p4 & 1) * 8;
  const int baseE = base_b + bb * 32, baseO = base_b + (1 - bb) * 32;

  f32x4 acc[10][2];
#pragma unroll
  for (int u = 0; u < 10; ++u)
#pragma unroll
    for (int rt = 0; rt < 2; ++rt) acc[u][rt] = (f32x4){0.f, 0.f, 0.f, 0.f};

  raw_barrier();
  for (int kt = 0; kt < G1_NK; ++kt) {
    const char* sS = smem + (kt % G1_NS) * G1_STG;
#pragma unroll
    for (int ks = 0; ks < 2; ++ks) {
      bf16x8 af[2];
#pragma unroll
      for (int rt = 0; rt < 2; ++rt) af[rt] = *(const bf16x8*)(sS + rt * 2048 + (a_off0 ^ (ks * 64)));
#pragma unroll
      for (int u = 0; u < 10; ++u) {
        const char* bp = sS + ks * 10240 + ((u & 1) ? baseO : baseE) + (u >> 1) * 64;
        bf16x4 lo = tr_read(bp);
        bf16x4 hi = tr_read(bp + 1280);
        bf16x8 bf = __builtin_shufflevector(lo, hi, 0, 1, 2, 3, 4, 5, 6, 7);
#pragma unroll
        for (int rt = 0; rt < 2; ++rt)
          acc[u][rt] = __builtin_amdgcn_mfma_f32_16x16x32_bf16(bf, af[rt], acc[u][rt], 0, 0, 0);
      }
    }
    if (kt + 1 < G1_NK) raw_barrier();
  }

  bool has_kv = false;
#pragma unroll
  for (int pp = 0; pp < 5; ++pp) {
    const int PG = 5 * nt + pp;
    if (PG < 16) {
#pragma unroll
      for (int rt = 0; rt < 2; ++rt) {
        int rowt = (m0 + 32 * cw + 16 * rt) >> 4;
#pragma unroll
        for (int mem = 0; mem < 2; ++mem) {
          f32x4 a = acc[pp + 5 * mem][rt];
          float v0 = phi_f(rbf(a[0])), v1 = phi_f(rbf(a[1])), v2 = phi_f(rbf(a[2])), v3 = phi_f(rbf(a[3]));
          u32x2 o = {pack2(v0, v1), pack2(v2, v3)};
          *(u32x2*)(pqF + (((size_t)rowt * 16 + PG) * 64 + l) * 8 + mem * 4) = o;
        }
      }
    } else if (PG >= 48) {
      const int gi = PG - 48;
#pragma unroll
      for (int rt = 0; rt < 2; ++rt) {
        int m = m0 + 32 * cw + 16 * rt + l15;
        float hv[4];
#pragma unroll
        for (int r = 0; r < 4; ++r) {
          float gate = rbf(acc[pp][rt][r]);
          float up = rbf(acc[pp + 5][rt][r]);
          float ge = rbf(gelu_f(gate));
          hv[r] = ge * up;
        }
        u32x2 o = {pack2(hv[0], hv[1]), pack2(hv[2], hv[3])};
        *(u32x2*)(hbuf + (size_t)m * F_DIM + 16 * gi + 4 * g) = o;
      }
    } else {
      has_kv = true;
    }
  }
  if (has_kv) {
    __syncthreads();
    int tz = threadIdx.x;
    asm volatile("" : "+v"(tz));
    const int g = (tz & 63) >> 4, l15 = tz & 15, q = l15 >> 2, p4 = tz & 3;
    char* scr = smem + cw * 2048;
    float* red = (float*)(smem + 16384);
    const bf16x8 ones = {0x3F80, 0x3F80, 0x3F80, 0x3F80, 0x3F80, 0x3F80, 0x3F80, 0x3F80};
#pragma unroll
    for (int pp = 0; pp < 5; ++pp) {
      const int PG = 5 * nt + pp;
      if (PG >= 16 && PG < 48) {
#pragma unroll
        for (int rt = 0; rt < 2; ++rt) {
          f32x4 a = acc[pp][rt], v = acc[pp + 5][rt];
          float k0 = phi_f(rbf(a[0])), k1 = phi_f(rbf(a[1])), k2 = phi_f(rbf(a[2])), k3 = phi_f(rbf(a[3]));
          u32x2 pk = {pack2(k0, k1), pack2(k2, k3)};
          u32x2 vv = {pack2(v[0], v[1]), pack2(v[2], v[3])};
          *(u32x2*)(scr + (16 * rt + l15) * 32 + g * 8) = pk;
          *(u32x2*)(scr + 1024 + (16 * rt + l15) * 32 + g * 8) = vv;
        }
        asm volatile("s_waitcnt lgkmcnt(0)" ::: "memory");
        const char* rp = scr + (8 * g + q) * 32 + p4 * 8;
        bf16x4 a_lo = tr_read(rp), a_hi = tr_read(rp + 128);
        bf16x4 b_lo = tr_read(rp + 1024), b_hi = tr_read(rp + 1024 + 128);
        bf16x8 af = __builtin_shufflevector(a_lo, a_hi, 0, 1, 2, 3, 4, 5, 6, 7);
        bf16x8 bf = __builtin_shufflevector(b_lo, b_hi, 0, 1, 2, 3, 4, 5, 6, 7);
        f32x4 kvacc = {0.f, 0.f, 0.f, 0.f}, zacc = {0.f, 0.f, 0.f, 0.f};
        kvacc = __builtin_amdgcn_mfma_f32_16x16x32_bf16(af, bf, kvacc, 0, 0, 0);
        zacc = __builtin_amdgcn_mfma_f32_16x16x32_bf16(af, ones, zacc, 0, 0, 0);
        float* rr = red + (cw * 5 + pp) * 272;
        *(f32x4*)(rr + l15 * 16 + 4 * g) = kvacc;
        if (l15 == 0) *(f32x4*)(rr + 256 + 4 * g) = zacc;
        asm volatile("s_waitcnt lgkmcnt(0)" ::: "memory");
      }
    }
    __syncthreads();
    for (int i = tid; i < 5 * 272; i += 512) {
      int pp = i / 272, e = i % 272;
      int PG = 5 * nt + pp;
      if (PG >= 16 && PG < 48) {
        float s = 0.f;
#pragma unroll
        for (int c = 0; c < 8; ++c) s += red[(c * 5 + pp) * 272 + e];
        __hip_atomic_store(kvp + ((size_t)mt * NHEAD + (PG - 16)) * 272 + e, s, __ATOMIC_RELAXED, __HIP_MEMORY_SCOPE_AGENT);
      }
    }
    WAIT_VM(0);
    __syncthreads();
    unsigned* flag = (unsigned*)(smem + 65536);
    if (tid == 0) {
      unsigned old = __hip_atomic_fetch_add(cnt + nt * 64, 1u, __ATOMIC_RELAXED, __HIP_MEMORY_SCOPE_AGENT);
      *flag = old;
    }
    __syncthreads();
    if (*flag == 7u) {
      for (int i = tid; i < 5 * 272; i += 512) {
        int pp = i / 272, e = i % 272;
        int PG = 5 * nt + pp;
        if (PG >= 16 && PG < 48) {
          int head = PG - 16;
          const float* src = kvp + (size_t)head * 272 + e;
          float v[8];
#pragma unroll
          for (int m = 0; m < 8; ++m)
            v[m] = __hip_atomic_load(src + (size_t)m * NHEAD * 272, __ATOMIC_RELAXED, __HIP_MEMORY_SCOPE_AGENT);
          float s = 0.f;
#pragma unroll
          for (int m = 0; m < 8; ++m) s += v[m];
          if (e < 256) kvT[head * 256 + e] = f2bf(s);
          else zb[head * 16 + (e - 256)] = f2bf(s);
        }
      }
    }
  }
}

__global__ __launch_bounds__(768) void g1_kernel(const u16* __restrict__ xn, const u16* __restrict__ wqkv,
                                                 const u16* __restrict__ wg, const u16* __restrict__ wu,
                                                 u16* __restrict__ pqF, u16* __restrict__ hbuf,
                                                 float* __restrict__ kvp, u16* __restrict__ kvT,
                                                 u16* __restrict__ zb, unsigned* __restrict__ cnt) {
  __shared__ __attribute__((aligned(16))) char smem[G1_NS * G1_STG];
  g1_body(xn, wqkv, wg, wu, pqF, hbuf, kvp, kvT, zb, cnt, smem);
}

#define G2_NS 6
#define G2_NP 36
#define G2_STAGE 16384
#define G2_OSLOTS 3
#define G2_OFF_RING 0
#define G2_OFF_O    (G2_NS * G2_STAGE)
#define G2_OFF_KV   (G2_OFF_O + G2_OSLOTS * 8192)
#define G2_OFF_Z    (G2_OFF_KV + 16384)
#define G2_LDS      (G2_OFF_Z + 1024)

__host__ __device__ constexpr int g2_bq(int kt) { return 3 * kt + 1; }
__host__ __device__ constexpr int g2_nB(int kt) { return g2_bq(kt) + 3; }
__host__ __device__ constexpr int g2_posS(int kt) { return g2_nB(kt) + kt; }
__host__ __device__ constexpr int g2_sidx(int p) {
  for (int kt = 0; kt < 8; ++kt) if (g2_posS(kt) == p) return kt;
  return -1;
}
__host__ __device__ constexpr int g2_bidx(int p) {
  int c = 0;
  for (int kt = 0; kt < 8; ++kt) if (g2_posS(kt) < p) ++c;
  return p - c;
}
__host__ __device__ constexpr int g2_hb(int b) { int c = 0; for (int kt = 0; kt < 8; ++kt) if (g2_bq(kt) < b) ++c; return 4 * c; }
__host__ __device__ constexpr int g2_cnt(int p) { return (g2_sidx(p) >= 0) ? 2 : 4; }
__host__ __device__ constexpr int g2_allowed(int s) {
  int last = (s + G2_NS - 1 < G2_NP - 1) ? (s + G2_NS - 1) : (G2_NP - 1);
  int a = 0;
  for (int t = s + 3; t <= last; ++t) a += g2_cnt(t);
  return a;
}

__device__ __forceinline__ float div_nr(float a, float b, float rb) {
  float q = a * rb;
  float e = __builtin_fmaf(-q, b, a);
  return __builtin_fmaf(e, rb, q);
}

__device__ __forceinline__ void g2_body(const u16* __restrict__ x, const u16* __restrict__ pq,
                                        const u16* __restrict__ hbuf, const u16* __restrict__ kvT,
                                        const u16* __restrict__ zb, const u16* __restrict__ wo,
                                        const u16* __restrict__ wd, u16* __restrict__ out, char* smem) {
  char* sR = smem + G2_OFF_RING;
  char* sO = smem + G2_OFF_O;
  const int b = blockIdx.x;
  const int xcd = b & 7, idx = b >> 3;
  const int mt = xcd * 4 + (idx >> 3);
  const int ntile = idx & 7;
  const int m0 = mt * 64, n0 = ntile * 64;
  const int tid = threadIdx.x, w = tid >> 6, l = tid & 63;
  const int g = l >> 4, l15 = l & 15;

  if (w >= 4) {
    const int lw = w - 4;
    const u16* aptr[2];
    const u16* bptr_o[2];
    const u16* bptr_d[2];
#pragma unroll
    for (int t = 0; t < 2; ++t) {
      int P = 2 * lw + t;
      int row = 8 * P + (l >> 3);
      int chunk = (l & 7) ^ ((row >> 1) & 7);
      aptr[t] = hbuf + (size_t)(m0 + row) * F_DIM + chunk * 8;
      int krow = 8 * P + (l >> 3);
      int fb = ((krow >> 1) & 1) | (((krow >> 3) & 1) << 1);
      int c = (l & 7) ^ (fb << 1);
      bptr_o[t] = wo + (size_t)krow * D_MODEL + n0 + c * 8;
      bptr_d[t] = wd + (size_t)krow * D_MODEL + n0 + c * 8;
    }
#pragma unroll
    for (int t = 0; t < 4; ++t)
      __builtin_amdgcn_global_load_lds((const unsigned*)(kvT + (size_t)(4 * t + lw) * 512 + l * 8),
                                       (lds_u32*)(smem + G2_OFF_KV + (4 * t + lw) * 1024 + l * 16), 16, 0, 0);
    if (lw == 0)
      __builtin_amdgcn_global_load_lds((const unsigned*)(zb + l * 8), (lds_u32*)(smem + G2_OFF_Z + l * 16), 16, 0, 0);

    auto stage = [&](int p, int si, int bi) {
      char* sA = sR + (p % G2_NS) * G2_STAGE;
      char* sB = sA + 8192;
#pragma unroll
      for (int t = 0; t < 2; ++t) {
        int P = 2 * lw + t;
        if (si < 0) {
          __builtin_amdgcn_global_load_lds((const unsigned*)(aptr[t] + bi * 64), (lds_u32*)(sA + P * 1024 + l * 16), 16, 0, 0);
          __builtin_amdgcn_global_load_lds((const unsigned*)(bptr_d[t] + (size_t)bi * 64 * D_MODEL), (lds_u32*)(sB + P * 1024 + l * 16), 16, 0, 0);
        } else {
          __builtin_amdgcn_global_load_lds((const unsigned*)(bptr_o[t] + (size_t)si * 64 * D_MODEL), (lds_u32*)(sB + P * 1024 + l * 16), 16, 0, 0);
        }
      }
    };
#define G2_STAGE_AT(p) stage(p, g2_sidx(p), g2_bidx(p))
    G2_STAGE_AT(0); G2_STAGE_AT(1); G2_STAGE_AT(2); G2_STAGE_AT(3); G2_STAGE_AT(4);
    wait_vm<g2_allowed(-1)>();
    raw_barrier();
#define G2_LSTEP(s)                                                       \
    {                                                                     \
      if (s + G2_NS - 1 < G2_NP) G2_STAGE_AT(s + G2_NS - 1);              \
      if (s + 1 < G2_NP) { wait_vm<g2_allowed(s)>(); raw_barrier(); }     \
    }
    G2_LSTEP(0) G2_LSTEP(1) G2_LSTEP(2) G2_LSTEP(3) G2_LSTEP(4) G2_LSTEP(5) G2_LSTEP(6) G2_LSTEP(7)
    G2_LSTEP(8) G2_LSTEP(9) G2_LSTEP(10) G2_LSTEP(11) G2_LSTEP(12) G2_LSTEP(13) G2_LSTEP(14) G2_LSTEP(15)
    G2_LSTEP(16) G2_LSTEP(17) G2_LSTEP(18) G2_LSTEP(19) G2_LSTEP(20) G2_LSTEP(21) G2_LSTEP(22) G2_LSTEP(23)
    G2_LSTEP(24) G2_LSTEP(25) G2_LSTEP(26) G2_LSTEP(27) G2_LSTEP(28) G2_LSTEP(29) G2_LSTEP(30) G2_LSTEP(31)
    G2_LSTEP(32) G2_LSTEP(33) G2_LSTEP(34) G2_LSTEP(35)
    return;
  }
  const int wr = w >> 1, wc = w & 1;
  const int a_off0 = (wr * 32 + l15) * 128 + ((g ^ (l15 >> 1)) * 16);
  const int q = l15 >> 2, p4 = l & 3;
  const int fB = (q >> 1) | ((g & 1) << 1);
  int b_off[2];
#pragma unroll
  for (int nt = 0; nt < 2; ++nt) {
    int c = 4 * wc + 2 * nt + (p4 >> 1);
    b_off[nt] = 8192 + (8 * g + q) * 128 + ((c ^ (fB << 1)) * 16) + (p4 & 1) * 8;
  }
  const int lrowA = 16 * w + l15;
  const char* kv_l = smem + G2_OFF_KV + l15 * 32 + g * 8;
  const char* z_l = smem + G2_OFF_Z + g * 8;
  char* sO_l = sO + lrowA * 128 + (g & 1) * 8;
  const int swzA = (lrowA >> 1) & 7;
  bf16x8 pqf2[NHEAD / 2];
#pragma unroll
  for (int hp = 0; hp < NHEAD / 2; ++hp)
    pqf2[hp] = *(const bf16x8*)(pq + ((((size_t)(m0 >> 4) + w) * 16 + hp) * 64 + l) * 8);

  f32x4 accs[2][2], accb[2][2];
#pragma unroll
  for (int a = 0; a < 2; ++a)
#pragma unroll
    for (int c = 0; c < 2; ++c) { accs[a][c] = (f32x4){0.f, 0.f, 0.f, 0.f}; accb[a][c] = (f32x4){0.f, 0.f, 0.f, 0.f}; }

  bf16x8 af[2][2][2], bfr[2][2][2];
  auto read_frags = [&](int p, int si, int buf) {
    const char* sS = sR + (p % G2_NS) * G2_STAGE;
    const char* sAx = (si >= 0) ? (sO + (si % G2_OSLOTS) * 8192) : sS;
#pragma unroll
    for (int ks = 0; ks < 2; ++ks) {
#pragma unroll
      for (int rt = 0; rt < 2; ++rt) af[buf][ks][rt] = *(const bf16x8*)(sAx + rt * 2048 + (a_off0 ^ (ks * 64)));
#pragma unroll
      for (int nt = 0; nt < 2; ++nt) {
        bf16x4 lo = tr_read(sS + ks * 4096 + b_off[nt]);
        bf16x4 hi = tr_read(sS + ks * 4096 + 512 + b_off[nt]);
        bfr[buf][ks][nt] = __builtin_shufflevector(lo, hi, 0, 1, 2, 3, 4, 5, 6, 7);
      }
    }
  };
  u32x2 xv[2][2];

  raw_barrier();
  read_frags(0, g2_sidx(0), 0);

#define G2_CSTEP(s)                                                                                    \
  {                                                                                                    \
    constexpr int cb = (s) & 1;                                                                        \
    constexpr int si = g2_sidx(s), bi = g2_bidx(s);                                                    \
    if (s + 1 < G2_NP) read_frags(s + 1, g2_sidx(s + 1), cb ^ 1);                                      \
    _Pragma("unroll") for (int ks = 0; ks < 2; ++ks)                                                   \
    _Pragma("unroll") for (int rt = 0; rt < 2; ++rt)                                                   \
    _Pragma("unroll") for (int nt = 0; nt < 2; ++nt) {                                                 \
      if (si >= 0)                                                                                     \
        accs[nt][rt] = __builtin_amdgcn_mfma_f32_16x16x32_bf16(bfr[cb][ks][nt], af[cb][ks][rt], accs[nt][rt], 0, 0, 0); \
      else                                                                                             \
        accb[nt][rt] = __builtin_amdgcn_mfma_f32_16x16x32_bf16(bfr[cb][ks][nt], af[cb][ks][rt], accb[nt][rt], 0, 0, 0); \
    }                                                                                                  \
    if (si < 0) {                                                                                      \
      constexpr int h0 = g2_hb(bi), h1 = g2_hb(bi + 1);                                                \
      if (h1 > h0) {                                                                                   \
        bf16x4 kvf[4], zf[4];                                                                          \
        f32x4 num[4], den[4];                                                                          \
        _Pragma("unroll") for (int hh = 0; hh < 4; ++hh) {                                             \
          kvf[hh] = *(const bf16x4*)(kv_l + (h0 + hh) * 512);                                          \
          zf[hh] = *(const bf16x4*)(z_l + (h0 + hh) * 32);                                             \
        }                                                                                              \
        _Pragma("unroll") for (int hh = 0; hh < 4; ++hh) {                                             \
          const int h = h0 + hh;                                                                       \
          bf16x4 pqh = (h & 1) ? __builtin_shufflevector(pqf2[h >> 1], pqf2[h >> 1], 4, 5, 6, 7)        \
                               : __builtin_shufflevector(pqf2[h >> 1], pqf2[h >> 1], 0, 1, 2, 3);       \
          num[hh] = __builtin_amdgcn_mfma_f32_16x16x16bf16_1k(kvf[hh], pqh, (f32x4){0.f, 0.f, 0.f, 0.f}, 0, 0, 0); \
          den[hh] = __builtin_amdgcn_mfma_f32_16x16x16bf16_1k(zf[hh], pqh, (f32x4){0.f, 0.f, 0.f, 0.f}, 0, 0, 0);  \
        }                                                                                              \
        __builtin_amdgcn_sched_barrier(0);                                                             \
        _Pragma("unroll") for (int hh = 0; hh < 4; ++hh) {                                             \
          const int h = h0 + hh;                                                                       \
          float dn = rbf(rbf(den[hh][0]) + 1e-6f);                                                     \
          float rd = __builtin_amdgcn_rcpf(dn);                                                        \
          float o0 = div_nr(rbf(num[hh][0]), dn, rd), o1 = div_nr(rbf(num[hh][1]), dn, rd);            \
          float o2 = div_nr(rbf(num[hh][2]), dn, rd), o3 = div_nr(rbf(num[hh][3]), dn, rd);            \
          u32x2 ov = {pack2(o0, o1), pack2(o2, o3)};                                                   \
          *(u32x2*)(sO_l + ((h >> 2) % G2_OSLOTS) * 8192 + ((((h & 3) * 2 + (g >> 1)) ^ swzA) * 16)) = ov; \
        }                                                                                              \
      }                                                                                                \
    }                                                                                                  \
    if (s == 30) {                                                                                     \
      _Pragma("unroll") for (int nt = 0; nt < 2; ++nt)                                                 \
      _Pragma("unroll") for (int rt = 0; rt < 2; ++rt)                                                 \
        xv[nt][rt] = *(const u32x2*)(x + (size_t)(m0 + wr * 32 + rt * 16 + l15) * D_MODEL + n0 + wc * 32 + nt * 16 + 4 * g); \
    }                                                                                                  \
    if (s + 1 < G2_NP) raw_barrier();                                                                  \
  }
  G2_CSTEP(0) G2_CSTEP(1) G2_CSTEP(2) G2_CSTEP(3) G2_CSTEP(4) G2_CSTEP(5) G2_CSTEP(6) G2_CSTEP(7)
  G2_CSTEP(8) G2_CSTEP(9) G2_CSTEP(10) G2_CSTEP(11) G2_CSTEP(12) G2_CSTEP(13) G2_CSTEP(14) G2_CSTEP(15)
  G2_CSTEP(16) G2_CSTEP(17) G2_CSTEP(18) G2_CSTEP(19) G2_CSTEP(20) G2_CSTEP(21) G2_CSTEP(22) G2_CSTEP(23)
  G2_CSTEP(24) G2_CSTEP(25) G2_CSTEP(26) G2_CSTEP(27) G2_CSTEP(28) G2_CSTEP(29) G2_CSTEP(30) G2_CSTEP(31)
  G2_CSTEP(32) G2_CSTEP(33) G2_CSTEP(34) G2_CSTEP(35)

#pragma unroll
  for (int nt = 0; nt < 2; ++nt)
#pragma unroll
    for (int rt = 0; rt < 2; ++rt) {
      int m = m0 + wr * 32 + rt * 16 + l15;
      int n = n0 + wc * 32 + nt * 16 + 4 * g;
      float res[4];
#pragma unroll
      for (int r = 0; r < 4; ++r) {
        float s = rbf(accs[nt][rt][r]);
        float bb = rbf(accb[nt][rt][r]);
        float gg = rbf(silu_f(bb));
        float t = rbf(s * gg);
        unsigned xw = xv[nt][rt][r >> 1];
        float xf = (r & 1) ? __uint_as_float(xw & 0xffff0000u) : __uint_as_float(xw << 16);
        res[r] = xf + t;
      }
      u32x2 o = {pack2(res[0], res[1]), pack2(res[2], res[3])};
      *(u32x2*)(out + (size_t)m * D_MODEL + n) = o;
    }
}

__global__ __launch_bounds__(512) void g2_kernel(const u16* __restrict__ x, const u16* __restrict__ pq,
                                                 const u16* __restrict__ hbuf, const u16* __restrict__ kvT,
                                                 const u16* __restrict__ zb, const u16* __restrict__ wo,
                                                 const u16* __restrict__ wd, u16* __restrict__ out) {
  __shared__ __attribute__((aligned(16))) char smem[G2_LDS];
  g2_body(x, pq, hbuf, kvT, zb, wo, wd, out, smem);
}

extern "C" void kernel_launch(void* const* d_in, const int* in_sizes, int n_in, void* d_out, int out_size,
                              void* d_ws, size_t ws_size, hipStream_t stream) {
  const u16* x = (const u16*)d_in[0];
  const u16* wqkv = (const u16*)d_in[1];
  const u16* wo = (const u16*)d_in[2];
  const u16* wg = (const u16*)d_in[4];
  const u16* wu = (const u16*)d_in[5];
  const u16* wd = (const u16*)d_in[6];
  u16* out = (u16*)d_out;
  char* ws = (char*)d_ws;
  u16* xn = (u16*)(ws + WS_XN);
  u16* pq = (u16*)(ws + WS_PQ);
  u16* hbuf = (u16*)(ws + WS_H);
  float* kvp = (float*)(ws + WS_KVP);
  u16* kvT = (u16*)(ws + WS_KVT);
  u16* zb = (u16*)(ws + WS_ZB);
  unsigned* cnt = (unsigned*)(ws + WS_CNT);

  ln_kernel<<<S_LEN / 4, 256, 0, stream>>>(x, xn, cnt);
  g1_kernel<<<256, 768, 0, stream>>>(xn, wqkv, wg, wu, pq, hbuf, kvp, kvT, zb, cnt);
  g2_kernel<<<256, 512, 0, stream>>>(x, pq, hbuf, kvT, zb, wo, wd, out);
}
```
